```python
import torch

def _softmax(x, axis=-1):
    return torch.softmax(x, dim=axis)
def _gelu(x):
    return torch.nn.functional.gelu(x, approximate="tanh")
def _silu(x):
    return torch.nn.functional.silu(x)
def _sigmoid(x):
    return torch.sigmoid(x)
def _layernorm(x, axis=-1, eps=1e-5):
    m = x.mean(dim=axis, keepdim=True)
    v = x.var(dim=axis, keepdim=True, unbiased=False)
    return (x - m) / torch.sqrt(v + eps)
def _scatter_add(tgt, idx, src, axis=0):
    return tgt.clone().index_add_(axis, idx, src)
def _segment_sum(src, seg, n):
    out = torch.zeros((n,) + src.shape[1:], dtype=src.dtype)
    return out.index_add_(0, seg, src)
def _pad(x, pw):
    flat = tuple(v for pair in reversed(pw) for v in pair)
    return torch.nn.functional.pad(x, flat)

kernel_name = 'standalone__paged_block_table_kv_pagedattention__decode'

def setup_inputs():

    _w = (lambda a: (lambda t: t.to(torch.bfloat16) if t.is_floating_point() else t)(torch.as_tensor(a)))
    B, Hq, Hkv, Sq, Skv, D = 1, 32, 8, 1, 32768, 128
    bs = 16
    nbps = max(1, Skv // bs)
    num_phys = B * nbps + 4
    bt = torch.randperm(num_phys)[: B * nbps].reshape(B, nbps).to(torch.int64)
    cl = (nbps * bs - (torch.arange(1, B + 1, dtype=torch.int64) * 2654435761) % max(1, nbps * bs // 2)).to(torch.int64)
    return {
        "q": _w((lambda *s: torch.randn(*s, dtype=torch.bfloat16) * 0.8)(B, Hq, Sq, D)),
        "k_cache": _w((lambda *s: torch.randn(*s, dtype=torch.bfloat16) * 0.8)(num_phys, bs, Hkv, D)),
        "v_cache": _w((lambda *s: torch.randn(*s, dtype=torch.bfloat16) * 0.05)(num_phys, bs, Hkv, D)),
        "block_table": _w(bt),
        "context_lens": _w(cl),
    }

def reference(q, k_cache, v_cache, block_table, context_lens):
    B, Hq, Sq, D = q.shape
    bs = k_cache.shape[1]
    Hkv = k_cache.shape[2]
    nbps = block_table.shape[1]
    Skv = nbps * bs
    bt = block_table.reshape(-1)
    k = (lambda x, *d: x.permute(*d))(k_cache[bt].reshape(B, Skv, Hkv, D), 0, 2, 1, 3)
    v = (lambda x, *d: x.permute(*d))(v_cache[bt].reshape(B, Skv, Hkv, D), 0, 2, 1, 3)
    g = Hq // Hkv
    qg = q.reshape(B, Hkv, g, Sq, D)
    scores = torch.einsum("bhgqd,bhkd->bhgqk", qg, k) * (D ** -0.5)
    pos = torch.arange(Skv)
    msk = pos[None, :] < context_lens[:, None]
    scores = torch.where(msk[:, None, None, None, :], scores, -1e9)
    p = _softmax(scores, axis=-1)
    out = torch.einsum("bhgqk,bhkd->bhgqd", p, v)
    return out.reshape(B, Hq, Sq, D)
```

```cpp
#define KV_AUX 2
#define HEAD_MINOR 1
#include <hip/hip_runtime.h>
#include <hip/hip_bf16.h>
#include <stdint.h>

typedef unsigned u32x4 __attribute__((ext_vector_type(4)));
typedef unsigned u32x2 __attribute__((ext_vector_type(2)));
typedef float f32x4 __attribute__((ext_vector_type(4)));
typedef short s16x8 __attribute__((ext_vector_type(8)));
typedef short s16x4 __attribute__((ext_vector_type(4)));
typedef __bf16 bf16x2 __attribute__((ext_vector_type(2)));

#define STAMP(i)
constexpr int HQ = 32, HKV = 8, G = 4, D = 128, BS = 16;
constexpr int NSPLIT = 32, NW = 8, NTHR = NW * 64;
constexpr int R = 5;
constexpr int PAGE_BYTES = BS * HKV * D * 2;
constexpr int TOK_BYTES = HKV * D * 2;
constexpr float C2 = 0.088388347648318440f * 1.4426950408889634f;
constexpr int JS = 20, HS = 8 * JS + 4, WS = G * HS;
constexpr size_t PO_BYTES = (size_t)HKV * NSPLIT * G * D * 4;

__device__ __forceinline__ f32x4 mfma32(u32x4 a, u32x4 b, f32x4 c) {
  return __builtin_amdgcn_mfma_f32_16x16x32_bf16(__builtin_bit_cast(s16x8, a), __builtin_bit_cast(s16x8, b), c, 0, 0, 0);
}
template <int CTRL> __device__ __forceinline__ float dppf(float x) {
  return __builtin_bit_cast(float, __builtin_amdgcn_mov_dpp(__builtin_bit_cast(int, x), CTRL, 0xf, 0xf, true));
}
template <int CTRL> __device__ __forceinline__ unsigned dppu(unsigned x) {
  return (unsigned)__builtin_amdgcn_mov_dpp((int)x, CTRL, 0xf, 0xf, true);
}
constexpr int ROR4 = 0x124, ROR8 = 0x128, SHL4 = 0x104, SHL8 = 0x108, SHL12 = 0x10C;
__device__ __forceinline__ float xrow_max(float x) {
  auto s = __builtin_amdgcn_permlane16_swap(__float_as_uint(x), __float_as_uint(x), false, false);
  x = fmaxf(__uint_as_float(s[0]), __uint_as_float(s[1]));
  auto t = __builtin_amdgcn_permlane32_swap(__float_as_uint(x), __float_as_uint(x), false, false);
  return fmaxf(__uint_as_float(t[0]), __uint_as_float(t[1]));
}
__device__ __forceinline__ float xrow_sum(float x) {
  auto s = __builtin_amdgcn_permlane16_swap(__float_as_uint(x), __float_as_uint(x), false, false);
  x = __uint_as_float(s[0]) + __uint_as_float(s[1]);
  auto t = __builtin_amdgcn_permlane32_swap(__float_as_uint(x), __float_as_uint(x), false, false);
  return __uint_as_float(t[0]) + __uint_as_float(t[1]);
}
__device__ __forceinline__ unsigned pk_bf16(float lo, float hi) {
  bf16x2 v = {(__bf16)lo, (__bf16)hi};
  return __builtin_bit_cast(unsigned, v);
}
__device__ __forceinline__ float diag_reduce(f32x4 d0, f32x4 d1, f32x4 d2, f32x4 d3) {
  float w0, w1, s;
  asm volatile(
      "s_nop 7\n\t"
      "v_add_f32_dpp %0, %5, %3 row_ror:8 row_mask:0xf bank_mask:0x1\n\t"
      "v_add_f32_dpp %0, %6, %4 row_ror:8 row_mask:0xf bank_mask:0x2\n\t"
      "v_add_f32_dpp %0, %11, %13 row_ror:8 row_mask:0xf bank_mask:0x4\n\t"
      "v_add_f32_dpp %0, %12, %14 row_ror:8 row_mask:0xf bank_mask:0x8\n\t"
      "v_add_f32_dpp %1, %9, %7 row_ror:8 row_mask:0xf bank_mask:0x1\n\t"
      "v_add_f32_dpp %1, %10, %8 row_ror:8 row_mask:0xf bank_mask:0x2\n\t"
      "v_add_f32_dpp %1, %15, %17 row_ror:8 row_mask:0xf bank_mask:0x4\n\t"
      "v_add_f32_dpp %1, %16, %18 row_ror:8 row_mask:0xf bank_mask:0x8\n\t"
      "v_add_f32_dpp %2, %0, %0 row_shl:4 row_mask:0xf bank_mask:0x5\n\t"
      "s_nop 0\n\t"
      "v_add_f32_dpp %2, %1, %1 row_shr:4 row_mask:0xf bank_mask:0xa\n\t"
      : "=&v"(w0), "=&v"(w1), "=&v"(s)
      : "v"(d0[0]), "v"(d0[1]), "v"(d0[2]), "v"(d0[3]),
        "v"(d1[0]), "v"(d1[1]), "v"(d1[2]), "v"(d1[3]),
        "v"(d2[0]), "v"(d2[1]), "v"(d2[2]), "v"(d2[3]),
        "v"(d3[0]), "v"(d3[1]), "v"(d3[2]), "v"(d3[3]));
  return s;
}

__device__ __forceinline__ __amdgpu_buffer_rsrc_t page_rsrc(const unsigned char* base, unsigned phys, int nrec) {
  return __builtin_amdgcn_make_buffer_rsrc((void*)(base + (size_t)phys * PAGE_BYTES), 0, nrec, 0x00020000);
}

__global__ void __launch_bounds__(NTHR, 2)
pa_split_kernel(const unsigned short* __restrict__ q, const unsigned char* __restrict__ kc, const unsigned char* __restrict__ vc,
                const int* __restrict__ bt, const int* __restrict__ lens, float* __restrict__ ws, int nbps) {
  __shared__ __attribute__((aligned(16))) float lds[NW * WS + NW * G * 2];
  STAMP(0);
  const int tid = threadIdx.x, lane = tid & 63;
  const int wave = __builtin_amdgcn_readfirstlane(tid >> 6);
  const int kvh = blockIdx.x & 7, sp = blockIdx.x >> 3;
  const int row = lane >> 4, n16 = lane & 15, bank = n16 >> 2, hh = n16 & 3;
  const int pg0 = sp + NSPLIT * wave;
  const int bidx = pg0 + (NSPLIT * NW) * lane;
  const int btv = bt[min(bidx, nbps - 1)];
  const int len_raw = lens[0];
  const u32x4 qf = *reinterpret_cast<const u32x4*>(q + (kvh * G + hh) * D + 32 * row + 8 * bank);
  const int skv = nbps * BS;
  const bool allmask = len_raw <= 0;
  const int len = allmask ? skv : min(len_raw, skv);
  const int npages = (len + BS - 1) / BS;
  const int n = (npages > pg0) ? (npages - pg0 + NSPLIT * NW - 1) / (NSPLIT * NW) : 0;
  const unsigned char* kb = kc + kvh * (D * 2);
  const unsigned char* vb = vc + kvh * (D * 2);
  const int koff = bank * TOK_BYTES + row * 64 + hh * 16;
  const int voff = row * TOK_BYTES + n16 * 16;
  const int tokl = 4 * bank + row;

  u32x4 K[R][4], V[R][4];
  auto nrec_of = [&](int slot) -> int {
    const int rem = len - (pg0 + NSPLIT * NW * slot) * BS;
    return max(rem, 0) * TOK_BYTES;
  };
  auto issue_k = [&](int r) {
#pragma unroll
    for (int j = 0; j < R; ++j) {
      const int slot = r * R + j;
      const unsigned phys = (unsigned)__builtin_amdgcn_readlane(btv, slot & 63);
      const __amdgpu_buffer_rsrc_t rs = page_rsrc(kb, phys, nrec_of(slot));
#pragma unroll
      for (int i = 0; i < 4; ++i) K[j][i] = __builtin_amdgcn_raw_buffer_load_b128(rs, koff + i * 4 * TOK_BYTES, 0, KV_AUX);
    }
    __builtin_amdgcn_sched_barrier(0);
  };
  auto issue_v = [&](int r) {
#pragma unroll
    for (int j = 0; j < R; ++j) {
      const int slot = r * R + j;
      const unsigned phys = (unsigned)__builtin_amdgcn_readlane(btv, slot & 63);
      const __amdgpu_buffer_rsrc_t rs = page_rsrc(vb, phys, nrec_of(slot));
#pragma unroll
      for (int i = 0; i < 4; ++i) V[j][i] = __builtin_amdgcn_raw_buffer_load_b128(rs, voff + i * 4 * TOK_BYTES, 0, KV_AUX);
    }
    __builtin_amdgcn_sched_barrier(0);
  };

  f32x4 acc[8];
#pragma unroll
  for (int j = 0; j < 8; ++j) acc[j] = f32x4{0.f, 0.f, 0.f, 0.f};
  float m = -INFINITY, l = 0.f;
  const int nr = (n + R - 1) / R;

  STAMP(1);
  issue_k(0);
  issue_v(0);
  STAMP(2);
  for (int r = 0;; ++r) {
    float s[R];
#pragma unroll
    for (int j = 0; j < R; ++j) {
      const f32x4 z = {0.f, 0.f, 0.f, 0.f};
      const f32x4 d0 = mfma32(K[j][0], qf, z), d1 = mfma32(K[j][1], qf, z), d2 = mfma32(K[j][2], qf, z), d3 = mfma32(K[j][3], qf, z);
      const float sc = diag_reduce(d0, d1, d2, d3);
      const int rem = len - (pg0 + NSPLIT * NW * (r * R + j)) * BS;
      const int live = allmask ? 0 : rem;
      const float fill = (allmask && rem > 0) ? 0.f : -INFINITY;
      s[j] = (tokl < live) ? sc : fill;
    }
    STAMP(3);
    issue_k(r + 1);
    float mx = s[0];
#pragma unroll
    for (int j = 1; j < R; ++j) mx = fmaxf(mx, s[j]);
    mx = fmaxf(mx, dppf<ROR4>(mx));
    mx = fmaxf(mx, dppf<ROR8>(mx));
    mx = xrow_max(mx);
    const float mn = fmaxf(m, mx);
    if (r > 0) {
      const float alpha = __builtin_amdgcn_exp2f((m - mn) * C2);
      l *= alpha;
#pragma unroll
      for (int j = 0; j < 8; ++j) acc[j] *= alpha;
    }
    m = mn;
    const float mc = (mn == -INFINITY) ? 0.f : -mn * C2;
    float p[R];
#pragma unroll
    for (int j = 0; j < R; ++j) { p[j] = __builtin_amdgcn_exp2f(fmaf(s[j], C2, mc)); l += p[j]; }
#pragma unroll
    for (int j = 0; j + 1 < R; j += 2) {
      const unsigned x = pk_bf16(p[j], p[j + 1]);
      const u32x4 pb = {x, dppu<SHL4>(x), dppu<SHL8>(x), dppu<SHL12>(x)};
#pragma unroll
      for (int e = 0; e < 8; ++e) {
        const unsigned sel = (e & 1) ? 0x07060302u : 0x05040100u;
        u32x4 a;
#pragma unroll
        for (int i = 0; i < 4; ++i) a[i] = __builtin_amdgcn_perm(V[j + 1][i][e >> 1], V[j][i][e >> 1], sel);
        acc[e] = mfma32(a, pb, acc[e]);
      }
    }
    if (R & 1) {
      constexpr int j = R - 1;
      const unsigned x = pk_bf16(p[j], dppf<SHL4>(p[j]));
      const u32x4 pb = {x, dppu<SHL8>(x), 0u, 0u};
#pragma unroll
      for (int e = 0; e < 8; ++e) {
        const unsigned sel = (e & 1) ? 0x07060302u : 0x05040100u;
        const u32x4 a = {__builtin_amdgcn_perm(V[j][1][e >> 1], V[j][0][e >> 1], sel),
                         __builtin_amdgcn_perm(V[j][3][e >> 1], V[j][2][e >> 1], sel), 0u, 0u};
        acc[e] = mfma32(a, pb, acc[e]);
      }
    }
    if (r + 1 >= nr) break;
    issue_v(r + 1);
  }
  STAMP(4);
  l += dppf<ROR4>(l);
  l += dppf<ROR8>(l);
  l = xrow_sum(l);
  float* ml = lds + NW * WS;
  if (n16 < G) {
    float* dst = lds + wave * WS + n16 * HS + 4 * row;
#pragma unroll
    for (int e = 0; e < 8; ++e) *reinterpret_cast<f32x4*>(dst + e * JS) = acc[e];
    if (row == 0) *reinterpret_cast<float2*>(ml + (wave * G + n16) * 2) = make_float2(m, l);
  }
  __syncthreads();
  STAMP(5);
  {
    const int h = tid >> 7, d = tid & 127;
    float mw[NW], lw[NW], M = -INFINITY;
#pragma unroll
    for (int w = 0; w < NW; ++w) { const float2 t = *reinterpret_cast<const float2*>(ml + (w * G + h) * 2); mw[w] = t.x; lw[w] = t.y; M = fmaxf(M, t.x); }
    const float Ms = (M == -INFINITY) ? 0.f : M;
    float L = 0.f, o = 0.f;
    const float* src = lds + h * HS + (d & 7) * JS + (d >> 3);
#pragma unroll
    for (int w = 0; w < NW; ++w) {
      const float wt = __builtin_amdgcn_exp2f((mw[w] - Ms) * C2);
      L = fmaf(wt, lw[w], L);
      o = fmaf(wt, src[w * WS], o);
    }
    const int slot = (kvh * NSPLIT + sp) * G + h;
    ws[(size_t)slot * D + d] = o;
    if (d == 0) reinterpret_cast<float2*>(reinterpret_cast<unsigned char*>(ws) + PO_BYTES)[slot] = make_float2(M, L);
  }
}

__global__ void __launch_bounds__(64)
pa_combine_kernel(const float* __restrict__ ws, unsigned short* __restrict__ out) {
  STAMP(0);
  const int qh = blockIdx.x >> 2, qd = blockIdx.x & 3, lane = threadIdx.x;
  const int sg = lane >> 3, e4 = lane & 7;
  const int kvh = qh >> 2, h = qh & 3;
  const float2* pml = reinterpret_cast<const float2*>(reinterpret_cast<const unsigned char*>(ws) + PO_BYTES);
  f32x4 v[4];
  float2 t[4];
#pragma unroll
  for (int i = 0; i < 4; ++i) {
    const int slot = (kvh * NSPLIT + sg * 4 + i) * G + h;
    v[i] = *reinterpret_cast<const f32x4*>(ws + (size_t)slot * D + qd * 32 + e4 * 4);
    t[i] = pml[slot];
  }
  float M = fmaxf(fmaxf(t[0].x, t[1].x), fmaxf(t[2].x, t[3].x));
  M = fmaxf(M, dppf<ROR8>(M));
  M = xrow_max(M);
  float L = 0.f;
  f32x4 o = {0.f, 0.f, 0.f, 0.f};
#pragma unroll
  for (int i = 0; i < 4; ++i) {
    const float wt = __builtin_amdgcn_exp2f((t[i].x - M) * C2);
    L = fmaf(wt, t[i].y, L);
    o += wt * v[i];
  }
  L += dppf<ROR8>(L); L = xrow_sum(L);
#pragma unroll
  for (int k = 0; k < 4; ++k) { float x = o[k]; x += dppf<ROR8>(x); o[k] = xrow_sum(x); }
  if (lane < 8) {
    const float inv = 1.f / L;
    u32x2 r = {pk_bf16(o[0] * inv, o[1] * inv), pk_bf16(o[2] * inv, o[3] * inv)};
    *reinterpret_cast<u32x2*>(out + qh * D + qd * 32 + e4 * 4) = r;
  }
}

extern "C" void kernel_launch(void* const* d_in, const int* in_sizes, int n_in,
                              void* d_out, int out_size, void* d_ws, size_t ws_size,
                              hipStream_t stream) {
  const int nbps = in_sizes[3];
  pa_split_kernel<<<HKV * NSPLIT, NTHR, 0, stream>>>((const unsigned short*)d_in[0], (const unsigned char*)d_in[1], (const unsigned char*)d_in[2],
                                                     (const int*)d_in[3], (const int*)d_in[4], (float*)d_ws, nbps);
  pa_combine_kernel<<<HQ * 4, 64, 0, stream>>>((const float*)d_ws, (unsigned short*)d_out);
}
```

```cpp
#define PADX 1200
#define PADY 0
#include <hip/hip_runtime.h>
#include <hip/hip_bf16.h>
#include <stdint.h>

typedef unsigned u32x4 __attribute__((ext_vector_type(4)));
typedef unsigned u32x2 __attribute__((ext_vector_type(2)));
typedef float f32x4 __attribute__((ext_vector_type(4)));
typedef short s16x8 __attribute__((ext_vector_type(8)));
typedef __bf16 bf16x2 __attribute__((ext_vector_type(2)));

#define STAMP(i)
#define KV_AUX 2
#define KW 3
#define KPF 1
#define POLL_SLEEP 0
#define FLAG_STRIDE 128
#define WARM_RED 0
#define WARM_PUB 0
#define WARM_OUT 0
#define AUX_POLL 16
#define AUX_PUB 16
#define NANN 2
#define FLAG_DELAY 5

constexpr int HKV = 8, G = 4, D = 128, BS = 16;
#define HPW 1
constexpr int NWG = 256, NGRP = HKV / HPW, NS = NWG / NGRP;
constexpr int NW = 8, NTHR = (NW + 1) * 64;
constexpr int WPH = NW / HPW;
static_assert(NS <= 64, "one ready word per lane");
constexpr int R = 5;
constexpr int WAVES_PER_HEAD = NS * WPH;
constexpr int PAGE_BYTES = BS * HKV * D * 2;
constexpr int TOK_BYTES = HKV * D * 2;
constexpr float C2 = 0.088388347648318440f * 1.4426950408889634f;
constexpr int JS = 20, HS = 8 * JS + 4, WS = G * HS;
constexpr int LDS_L = NW * WS, LDS_MF = LDS_L + NW * G, LDS_MR = LDS_MF + HPW * G, LDS_FLOATS = LDS_MR + 2 * NW * G;
constexpr int MSG = 128, BOX = NS * MSG, FLAGS_OFF = NWG * BOX, FSTR = FLAG_STRIDE, WS_USED = FLAGS_OFF + NWG * FSTR;
constexpr int AUX_SC1 = 16;
constexpr unsigned SPIN_CAP = 1u << 18;
constexpr unsigned OOB = 0x80000000u;

__device__ __forceinline__ f32x4 mfma32(u32x4 a, u32x4 b, f32x4 c) {
  return __builtin_amdgcn_mfma_f32_16x16x32_bf16(__builtin_bit_cast(s16x8, a), __builtin_bit_cast(s16x8, b), c, 0, 0, 0);
}
template <int CTRL> __device__ __forceinline__ float dppf(float x) {
  return __builtin_bit_cast(float, __builtin_amdgcn_mov_dpp(__builtin_bit_cast(int, x), CTRL, 0xf, 0xf, true));
}
template <int CTRL> __device__ __forceinline__ unsigned dppu(unsigned x) {
  return (unsigned)__builtin_amdgcn_mov_dpp((int)x, CTRL, 0xf, 0xf, true);
}
constexpr int ROR4 = 0x124, ROR8 = 0x128, SHL4 = 0x104, SHL8 = 0x108, SHL12 = 0x10C;
__device__ __forceinline__ float xrow_max(float x) {
  auto s = __builtin_amdgcn_permlane16_swap(__float_as_uint(x), __float_as_uint(x), false, false);
  x = fmaxf(__uint_as_float(s[0]), __uint_as_float(s[1]));
  auto t = __builtin_amdgcn_permlane32_swap(__float_as_uint(x), __float_as_uint(x), false, false);
  return fmaxf(__uint_as_float(t[0]), __uint_as_float(t[1]));
}
__device__ __forceinline__ float xrow_sum(float x) {
  auto s = __builtin_amdgcn_permlane16_swap(__float_as_uint(x), __float_as_uint(x), false, false);
  x = __uint_as_float(s[0]) + __uint_as_float(s[1]);
  auto t = __builtin_amdgcn_permlane32_swap(__float_as_uint(x), __float_as_uint(x), false, false);
  return __uint_as_float(t[0]) + __uint_as_float(t[1]);
}
__device__ __forceinline__ unsigned pk_bf16(float lo, float hi) {
  bf16x2 v = {(__bf16)lo, (__bf16)hi};
  return __builtin_bit_cast(unsigned, v);
}
__device__ __forceinline__ float diag_reduce(f32x4 d0, f32x4 d1, f32x4 d2, f32x4 d3) {
  float w0, w1, s;
  asm volatile(
      "s_nop 7\n\t"
      "v_add_f32_dpp %0, %5, %3 row_ror:8 row_mask:0xf bank_mask:0x1\n\t"
      "v_add_f32_dpp %0, %6, %4 row_ror:8 row_mask:0xf bank_mask:0x2\n\t"
      "v_add_f32_dpp %0, %11, %13 row_ror:8 row_mask:0xf bank_mask:0x4\n\t"
      "v_add_f32_dpp %0, %12, %14 row_ror:8 row_mask:0xf bank_mask:0x8\n\t"
      "v_add_f32_dpp %1, %9, %7 row_ror:8 row_mask:0xf bank_mask:0x1\n\t"
      "v_add_f32_dpp %1, %10, %8 row_ror:8 row_mask:0xf bank_mask:0x2\n\t"
      "v_add_f32_dpp %1, %15, %17 row_ror:8 row_mask:0xf bank_mask:0x4\n\t"
      "v_add_f32_dpp %1, %16, %18 row_ror:8 row_mask:0xf bank_mask:0x8\n\t"
      "v_add_f32_dpp %2, %0, %0 row_shl:4 row_mask:0xf bank_mask:0x5\n\t"
      "s_nop 0\n\t"
      "v_add_f32_dpp %2, %1, %1 row_shr:4 row_mask:0xf bank_mask:0xa\n\t"
      : "=&v"(w0), "=&v"(w1), "=&v"(s)
      : "v"(d0[0]), "v"(d0[1]), "v"(d0[2]), "v"(d0[3]),
        "v"(d1[0]), "v"(d1[1]), "v"(d1[2]), "v"(d1[3]),
        "v"(d2[0]), "v"(d2[1]), "v"(d2[2]), "v"(d2[3]),
        "v"(d3[0]), "v"(d3[1]), "v"(d3[2]), "v"(d3[3]));
  return s;
}

__global__ void __launch_bounds__(NTHR, 3)
pa_decode_kernel(const unsigned short* __restrict__ q, const unsigned char* __restrict__ kc, const unsigned char* __restrict__ vc,
                 const int* __restrict__ bt, const int* __restrict__ lens, unsigned char* ws, unsigned short* __restrict__ out,
                 int nbps, int kvbytes) {
  __shared__ __attribute__((aligned(16))) float lds[LDS_FLOATS];
  unsigned long long tstart;
  asm volatile("s_memrealtime %0\n\ts_waitcnt lgkmcnt(0)" : "=s"(tstart) :: "memory");
  __builtin_amdgcn_sched_barrier(0);
  asm volatile("" :: "s"(q), "s"(kc), "s"(vc), "s"(bt), "s"(lens), "s"(ws), "s"(out), "s"(nbps), "s"(kvbytes));
  const int tid = threadIdx.x, lane = tid & 63;
  const int wave = __builtin_amdgcn_readfirstlane(tid >> 6);
  const int grp = blockIdx.x % NGRP, sp = blockIdx.x / NGRP;
  const __amdgpu_buffer_rsrc_t wsr = __builtin_amdgcn_make_buffer_rsrc(ws, 0, WS_USED, 0x00020000);
  const int skv = nbps * BS;

  if (wave == 0) {
    const int mybox = (grp * NS + sp) * BOX, fline = FLAGS_OFF + grp * NS * FSTR;
    const unsigned snap = __builtin_amdgcn_raw_buffer_load_b32(wsr, (lane & (NS - 1)) * FSTR, fline, AUX_SC1);
    __builtin_amdgcn_sched_barrier(0);
#pragma unroll
    for (int i = 0; i < BOX / 1024; ++i) __builtin_amdgcn_raw_buffer_store_b128(u32x4{0u, 0u, 0u, 0u}, wsr, lane * 16 + i * 1024, mybox, AUX_SC1);
    const int len_raw = lens[0];
    asm volatile("s_waitcnt vmcnt(0)" ::: "memory");
    const int len = len_raw <= 0 ? skv : min(len_raw, skv);
    const int nunits = (len + 3) >> 2;
    const int nmax = (nunits > sp) ? (nunits - sp + WAVES_PER_HEAD - 1) / WAVES_PER_HEAD : 0;
    const int nr = max((nmax + 4 * R - 1) / (4 * R), 1);
    unsigned mine = __builtin_amdgcn_readlane(snap, sp);
    auto announce = [&]() { ++mine; if (lane == 0) __builtin_amdgcn_raw_buffer_store_b32(mine, wsr, sp * FSTR, fline, AUX_SC1); };
    auto all_ready = [&]() -> bool {
      asm volatile("" ::: "memory");
      const unsigned cur = __builtin_amdgcn_raw_buffer_load_b32(wsr, (lane & (NS - 1)) * FSTR, fline, AUX_SC1);
      return __all(cur != snap || (lane & (NS - 1)) == sp);
    };
#pragma unroll 1
    for (int a = 0; a < NANN; ++a) {
#pragma unroll 1
      for (int i = min(nmax, 4 * R); i > 0; --i) __builtin_amdgcn_s_sleep(FLAG_DELAY);
      announce();
    }
    for (int r = 0; r < nr; ++r) __builtin_amdgcn_s_barrier();
#pragma unroll 1
    for (unsigned spins = 0; spins < SPIN_CAP; ++spins) {
      if (all_ready()) break;
      if ((spins & 7u) == 0u) announce();
      __builtin_amdgcn_s_sleep(8);
    }
    __builtin_amdgcn_s_barrier();
    u32x4 v[NS / 16];
    u32x2 t[NS / 16];
#pragma unroll
    for (int i = 0; i < NS / 16; ++i) { v[i] = u32x4{0u, 0u, 0u, 0u}; t[i] = u32x2{0u, 0u}; }
    u32x2 res = {0u, 0u};
    auto reduce = [&]() {
      float M = __uint_as_float(t[0][0]);
#pragma unroll
      for (int i = 1; i < NS / 16; ++i) M = fmaxf(M, __uint_as_float(t[i][0]));
      M = fmaxf(M, dppf<ROR4>(M));
      M = fmaxf(M, dppf<ROR8>(M));
      M = xrow_max(M);
      float L = 0.f;
      f32x4 o = {0.f, 0.f, 0.f, 0.f};
#pragma unroll
      for (int i = 0; i < NS / 16; ++i) {
        const float wt = __builtin_amdgcn_exp2f((__uint_as_float(t[i][0]) - M) * C2);
        L = fmaf(wt, __uint_as_float(t[i][1]), L);
        o += wt * __builtin_bit_cast(f32x4, v[i]);
      }
      L += dppf<ROR4>(L); L += dppf<ROR8>(L); L = xrow_sum(L);
#pragma unroll
      for (int k = 0; k < 4; ++k) { float x = o[k]; x += dppf<ROR4>(x); x += dppf<ROR8>(x); o[k] = xrow_sum(x); }
      const float inv = __builtin_amdgcn_rcpf(L);
      res = u32x2{pk_bf16(o[0] * inv, o[1] * inv), pk_bf16(o[2] * inv, o[3] * inv)};
    };
#pragma unroll 1
    for (unsigned spins = 0; spins < SPIN_CAP; ++spins) {
      asm volatile("" ::: "memory");
      bool ok = true;
#pragma unroll
      for (int i = 0; i < NS / 16; ++i)
        if (((v[i][0] & v[i][1] & v[i][2] & v[i][3] & t[i][0] & t[i][1]) & 1u) == 0u) {
          v[i] = __builtin_amdgcn_raw_buffer_load_b128(wsr, (lane >> 2) * MSG + (lane & 3) * 16 + i * 16 * MSG, mybox, AUX_POLL);
          t[i] = __builtin_amdgcn_raw_buffer_load_b64(wsr, (lane >> 2) * MSG + 64 + i * 16 * MSG, mybox, AUX_POLL);
        }
#pragma unroll
      for (int i = 0; i < NS / 16; ++i) ok &= ((v[i][0] & v[i][1] & v[i][2] & v[i][3] & t[i][0] & t[i][1]) & 1u) != 0u;
      if (__all(ok)) break;
      if ((spins & 31u) == 31u) announce();
      __builtin_amdgcn_s_sleep(POLL_SLEEP);
    }
    STAMP(5);
    reduce();
    STAMP(3);
    if (lane < 4) *reinterpret_cast<u32x2*>(out + grp * (HPW * G * D) + sp * 16 + lane * 4) = res;
  } else {
    const int sw = wave - 1, hl = sw % HPW, sh = sw / HPW;
    const int kvh = grp * HPW + hl;
    const int row = lane >> 4, n16 = lane & 15, bank = n16 >> 2, hh = n16 & 3;
    const int gw = sp + NS * sh;
    const int btv = bt[(unsigned)min((gw >> 2) + (WAVES_PER_HEAD / 4) * lane, nbps - 1)];
    const int len_raw = lens[0];
    const u32x4 qf = *reinterpret_cast<const u32x4*>(q + (unsigned)((kvh * G + hh) * D + 32 * row + 8 * bank));
    const bool allmask = len_raw <= 0;
    const int len = allmask ? skv : min(len_raw, skv);
    const int nunits = (len + 3) >> 2;
    const int nmax = (nunits > sp) ? (nunits - sp + WAVES_PER_HEAD - 1) / WAVES_PER_HEAD : 0;
    const int nr = (nmax + 4 * R - 1) / (4 * R);
    const unsigned offv = (gw + WAVES_PER_HEAD * lane < nunits) ? (unsigned)btv * PAGE_BYTES : OOB;
    const int shift = kvh * (D * 2) + (gw & 3) * (4 * TOK_BYTES);
    const __amdgpu_buffer_rsrc_t krs = __builtin_amdgcn_make_buffer_rsrc((void*)(kc + shift), 0, kvbytes - shift, 0x00020000);
    const __amdgpu_buffer_rsrc_t vrs = __builtin_amdgcn_make_buffer_rsrc((void*)(vc + shift), 0, kvbytes - shift, 0x00020000);
    const int koff = bank * TOK_BYTES + row * 64 + hh * 16;
    const int voff = row * TOK_BYTES + n16 * 16;
    const int tokl = 4 * (gw + WAVES_PER_HEAD * bank) + row;

    u32x4 K[R][4], V[R][4];
    unsigned so[4 * R];
    auto get_so = [&](int r) {
#pragma unroll
      for (int k = 0; k < 4 * R; ++k) so[k] = __builtin_amdgcn_readlane(offv, (r * 4 * R + k) & 63);
    };
    auto ldk = [&](int j) {
#pragma unroll
      for (int i = 0; i < 4; ++i) K[j][i] = __builtin_amdgcn_raw_buffer_load_b128(krs, koff, so[4 * j + i], KV_AUX);
    };
    auto ldv = [&](int j) {
#pragma unroll
      for (int i = 0; i < 4; ++i) V[j][i] = __builtin_amdgcn_raw_buffer_load_b128(vrs, voff, so[4 * j + i], KV_AUX);
    };
    f32x4 acc[8];
#pragma unroll
    for (int j = 0; j < 8; ++j) acc[j] = f32x4{0.f, 0.f, 0.f, 0.f};
    float m = -INFINITY, l = 0.f;

    get_so(0);
    asm volatile("" :: "s"(so[0]));
    __builtin_amdgcn_sched_barrier(0);
    while (__builtin_amdgcn_s_memrealtime() - tstart < PADY) __builtin_amdgcn_s_sleep(1);
    __builtin_amdgcn_sched_barrier(0);
#pragma unroll
    for (int j = 0; j < KPF; ++j) ldk(j);
    for (int r = 0;; ++r) {
      const bool last = r + 1 >= nr;
#pragma unroll
      for (int j = KPF; j < KW; ++j) ldk(j);
      __builtin_amdgcn_sched_barrier(0);
      float s[R];
#pragma unroll
      for (int j = 0; j < R; ++j) {
        const f32x4 z = {0.f, 0.f, 0.f, 0.f};
        const f32x4 d0 = mfma32(K[j][0], qf, z), d1 = mfma32(K[j][1], qf, z), d2 = mfma32(K[j][2], qf, z), d3 = mfma32(K[j][3], qf, z);
        __builtin_amdgcn_sched_barrier(0);
        if (j + KW < R) ldk(j + KW); else ldv(j + KW - R);
        if (j == R - 1) {
#pragma unroll
          for (int jj = KW; jj < R; ++jj) ldv(jj);
        }
        __builtin_amdgcn_sched_barrier(0);
        float sc = diag_reduce(d0, d1, d2, d3);
        sc = allmask ? 0.f : sc;
        s[j] = (tokl < len - (r * R + j) * (16 * WAVES_PER_HEAD)) ? sc : -INFINITY;
      }
      float mx = s[0];
#pragma unroll
      for (int j = 1; j < R; ++j) mx = fmaxf(mx, s[j]);
      mx = fmaxf(mx, dppf<ROR4>(mx));
      mx = fmaxf(mx, dppf<ROR8>(mx));
      mx = xrow_max(mx);
      float* mr = lds + LDS_MR + (r & 1) * (NW * G) + hl * (G * WPH);
      if (lane < G) mr[lane * WPH + sh] = mx;
      if (!last) {
        get_so(r + 1);
#pragma unroll
        for (int j = 0; j < KPF; ++j) ldk(j);
      }
      __syncthreads();
      float mn = m;
#pragma unroll
      for (int w = 0; w < WPH; ++w) mn = fmaxf(mn, mr[hh * WPH + w]);
      const float ms = (mn == -INFINITY) ? 0.f : mn;
      if (r > 0) {
        const float alpha = __builtin_amdgcn_exp2f((m - ms) * C2);
        l *= alpha;
#pragma unroll
        for (int j = 0; j < 8; ++j) acc[j] *= alpha;
      }
      m = mn;
      const float mc = -ms * C2;
      float p[R];
#pragma unroll
      for (int j = 0; j < R; ++j) { p[j] = __builtin_amdgcn_exp2f(fmaf(s[j], C2, mc)); l += p[j]; }
      if (last) {
        l += dppf<ROR4>(l);
        l += dppf<ROR8>(l);
        l = xrow_sum(l);
        if (lane < G) { lds[LDS_L + (hl * G + lane) * WPH + sh] = l; lds[LDS_MF + hl * G + lane] = fmaxf(m, -1e30f); }
      }
#pragma unroll
      for (int j = 0; j + 1 < R; j += 2) {
        const unsigned x = pk_bf16(p[j], p[j + 1]);
        const u32x4 pb = {x, dppu<SHL4>(x), dppu<SHL8>(x), dppu<SHL12>(x)};
#pragma unroll
        for (int e = 0; e < 8; ++e) {
          const unsigned sel = (e & 1) ? 0x07060302u : 0x05040100u;
          u32x4 a;
#pragma unroll
          for (int i = 0; i < 4; ++i) a[i] = __builtin_amdgcn_perm(V[j + 1][i][e >> 1], V[j][i][e >> 1], sel);
          acc[e] = mfma32(a, pb, acc[e]);
        }
      }
      if (R & 1) {
        constexpr int j = R - 1;
        const unsigned x = pk_bf16(p[j], dppf<SHL4>(p[j]));
        const u32x4 pb = {x, dppu<SHL8>(x), 0u, 0u};
#pragma unroll
        for (int e = 0; e < 8; ++e) {
          const unsigned sel = (e & 1) ? 0x07060302u : 0x05040100u;
          const u32x4 a = {__builtin_amdgcn_perm(V[j][1][e >> 1], V[j][0][e >> 1], sel),
                           __builtin_amdgcn_perm(V[j][3][e >> 1], V[j][2][e >> 1], sel), 0u, 0u};
          acc[e] = mfma32(a, pb, acc[e]);
        }
      }
      if (last) break;
    }
    while (__builtin_amdgcn_s_memrealtime() - tstart < PADX) __builtin_amdgcn_s_sleep(1);
    if (n16 < G) {
      float* dst = lds + (hl * WPH + sh) * WS + n16 * HS + 4 * row;
#pragma unroll
      for (int e = 0; e < 8; ++e) *reinterpret_cast<f32x4*>(dst + e * JS) = acc[e];
    }
    const int t = tid - 64, h = t >> 7, d = t & 127;
    const float* src = lds + h * HS + (d & 7) * JS + (d >> 3);
    const bool isml = (t & 15) == 1;
    const int dst = (grp * NS + (t >> 4)) * BOX + sp * MSG + (isml ? 64 : (t & 12) * 4);
    int dsto = ((t & 3) == 0 || isml) ? dst : (int)OOB;
    asm volatile("" : "+v"(dsto));
    __syncthreads();
#pragma unroll
    for (int u = 0; u < HPW; ++u) {
      float o = src[u * WPH * WS], L = lds[LDS_L + (u * G + h) * WPH];
#pragma unroll
      for (int w = 1; w < WPH; ++w) { o += src[(u * WPH + w) * WS]; L += lds[LDS_L + (u * G + h) * WPH + w]; }
      const unsigned mb = __float_as_uint(lds[LDS_MF + u * G + h]) | 1u, lb = __float_as_uint(L) | 1u;
      const unsigned ob = __float_as_uint(o) | 1u;
      u32x4 v4 = {ob, dppu<0x55>(ob), dppu<0xAA>(ob), dppu<0xFF>(ob)};
      if (isml) v4 = u32x4{mb, lb, mb, lb};
      __builtin_amdgcn_raw_buffer_store_b128(v4, wsr, dsto, u * 32 * BOX, AUX_PUB);
    }
    STAMP(6);
  }
}

extern "C" void kernel_launch(void* const* d_in, const int* in_sizes, int n_in,
                              void* d_out, int out_size, void* d_ws, size_t ws_size,
                              hipStream_t stream) {
  const int nbps = in_sizes[3];
  pa_decode_kernel<<<NWG, NTHR, 0, stream>>>((const unsigned short*)d_in[0], (const unsigned char*)d_in[1], (const unsigned char*)d_in[2],
                                             (const int*)d_in[3], (const int*)d_in[4], (unsigned char*)d_ws, (unsigned short*)d_out,
                                             nbps, in_sizes[1] * 2);
}
```

```cpp
#include <hip/hip_runtime.h>
#include <hip/hip_bf16.h>
#include <stdint.h>

typedef unsigned u32x4 __attribute__((ext_vector_type(4)));
typedef unsigned u32x2 __attribute__((ext_vector_type(2)));
typedef float f32x4 __attribute__((ext_vector_type(4)));
typedef short s16x8 __attribute__((ext_vector_type(8)));
typedef __bf16 bf16x2 __attribute__((ext_vector_type(2)));
#define LAS __attribute__((address_space(3)))
#define CAS __attribute__((address_space(4)))

constexpr int HKV = 8, G = 4, D = 128, BS = 16;
constexpr int NWG = 256, NS = NWG / HKV;
constexpr int NW = 8, NTHR = (NW + 1) * 64;
constexpr int NU = 18;
constexpr int R = (NU + 3) / 4;
static_assert(NU > 16 && NU <= 20, "P.V schedule: two full groups of 8 units + a rest of 1..4");
constexpr int WAVES_PER_HEAD = NS * NW;
constexpr int PAGE_BYTES = BS * HKV * D * 2;
constexpr int TOK_BYTES = HKV * D * 2;
constexpr float C2 = 0.088388347648318440f * 1.4426950408889634f;
constexpr int JS = 20, HS = 8 * JS + 4, WS = G * HS;
constexpr int LDS_M = NW * WS, LDS_L = LDS_M + NW * G, LDS_PH = LDS_L + NW * G, LDS_FLOATS = LDS_PH + NS * 5 * 4;
constexpr int MSG = 128, BOX = NS * MSG, FLAGS_OFF = NWG * BOX, FSTR = 128, WS_USED = FLAGS_OFF + NWG * FSTR;
constexpr int WINQ = 12;
constexpr int FLAG_DELAY = 6;
constexpr int GATHER_DELAY = 26;
constexpr int KV_AUX = 2;
constexpr int AUX_SC1 = 16;
constexpr unsigned SPIN_CAP = 1u << 18;
constexpr unsigned OOB = 0x80000000u;

constexpr int issued_after(int s) { return s + 1 + (WINQ + ((s + 4) & 3)) / 4; }
constexpr int pv_done_after(int i) { return i < 7 ? 0 : i < 15 ? i - 6 : i == NU - 1 ? 24 : i == 15 ? 12 : 16; }
__device__ __forceinline__ f32x4 mfma32(u32x4 a, u32x4 b, f32x4 c) {
  return __builtin_amdgcn_mfma_f32_16x16x32_bf16(__builtin_bit_cast(s16x8, a), __builtin_bit_cast(s16x8, b), c, 0, 0, 0);
}
template <int CTRL> __device__ __forceinline__ float dppf(float x) {
  return __builtin_bit_cast(float, __builtin_amdgcn_mov_dpp(__builtin_bit_cast(int, x), CTRL, 0xf, 0xf, true));
}
template <int CTRL> __device__ __forceinline__ unsigned dppu(unsigned x) {
  return (unsigned)__builtin_amdgcn_mov_dpp((int)x, CTRL, 0xf, 0xf, true);
}
constexpr int ROR4 = 0x124, ROR8 = 0x128, SHL4 = 0x104, SHL8 = 0x108, SHL12 = 0x10C;
__device__ __forceinline__ float xrow_max(float x) {
  auto s = __builtin_amdgcn_permlane16_swap(__float_as_uint(x), __float_as_uint(x), false, false);
  x = fmaxf(__uint_as_float(s[0]), __uint_as_float(s[1]));
  auto t = __builtin_amdgcn_permlane32_swap(__float_as_uint(x), __float_as_uint(x), false, false);
  return fmaxf(__uint_as_float(t[0]), __uint_as_float(t[1]));
}
__device__ __forceinline__ float xrow_sum(float x) {
  auto s = __builtin_amdgcn_permlane16_swap(__float_as_uint(x), __float_as_uint(x), false, false);
  x = __uint_as_float(s[0]) + __uint_as_float(s[1]);
  auto t = __builtin_amdgcn_permlane32_swap(__float_as_uint(x), __float_as_uint(x), false, false);
  return __uint_as_float(t[0]) + __uint_as_float(t[1]);
}
__device__ __forceinline__ float max3(float a, float b, float c) {
  float r;
  asm("v_max3_f32 %0, %1, %2, %3" : "=v"(r) : "v"(a), "v"(b), "v"(c));
  return r;
}
__device__ __forceinline__ unsigned pk_bf16(float lo, float hi) {
  bf16x2 v = {(__bf16)lo, (__bf16)hi};
  return __builtin_bit_cast(unsigned, v);
}
__device__ __forceinline__ float diag_reduce(f32x4 d0, f32x4 d1, f32x4 d2, f32x4 d3) {
  float w0, w1, s;
  asm volatile(
      "s_nop 7\n\t"
      "v_add_f32_dpp %0, %5, %3 row_ror:8 row_mask:0xf bank_mask:0x1\n\t"
      "v_add_f32_dpp %0, %6, %4 row_ror:8 row_mask:0xf bank_mask:0x2\n\t"
      "v_add_f32_dpp %0, %11, %13 row_ror:8 row_mask:0xf bank_mask:0x4\n\t"
      "v_add_f32_dpp %0, %12, %14 row_ror:8 row_mask:0xf bank_mask:0x8\n\t"
      "v_add_f32_dpp %1, %9, %7 row_ror:8 row_mask:0xf bank_mask:0x1\n\t"
      "v_add_f32_dpp %1, %10, %8 row_ror:8 row_mask:0xf bank_mask:0x2\n\t"
      "v_add_f32_dpp %1, %15, %17 row_ror:8 row_mask:0xf bank_mask:0x4\n\t"
      "v_add_f32_dpp %1, %16, %18 row_ror:8 row_mask:0xf bank_mask:0x8\n\t"
      "v_add_f32_dpp %2, %0, %0 row_shl:4 row_mask:0xf bank_mask:0x5\n\t"
      "s_nop 0\n\t"
      "v_add_f32_dpp %2, %1, %1 row_shr:4 row_mask:0xf bank_mask:0xa\n\t"
      : "=&v"(w0), "=&v"(w1), "=&v"(s)
      : "v"(d0[0]), "v"(d0[1]), "v"(d0[2]), "v"(d0[3]),
        "v"(d1[0]), "v"(d1[1]), "v"(d1[2]), "v"(d1[3]),
        "v"(d2[0]), "v"(d2[1]), "v"(d2[2]), "v"(d2[3]),
        "v"(d3[0]), "v"(d3[1]), "v"(d3[2]), "v"(d3[3]));
  return s;
}

__global__ void __launch_bounds__(NTHR, 3)
pa_decode_kernel(const unsigned short* __restrict__ q, const unsigned char* __restrict__ kc, const unsigned char* __restrict__ vc,
                 const int* __restrict__ bt, const int* __restrict__ lens, unsigned char* ws, unsigned short* __restrict__ out,
                 int nbps, int kvbytes) {
  __shared__ __attribute__((aligned(16))) float lds[LDS_FLOATS];
  const int tid = threadIdx.x, lane = tid & 63;
  const int wave = __builtin_amdgcn_readfirstlane(tid >> 6);
  const int kvh = blockIdx.x % HKV, sp = blockIdx.x / HKV;
  const int sw = wave - 1, gw = sp + NS * sw;
  int pgi = (gw >> 2) + (WAVES_PER_HEAD / 4) * lane;
  asm volatile("" : "+v"(pgi));
  __builtin_amdgcn_sched_barrier(0);
  asm volatile("" :: "s"(q), "s"(kc), "s"(vc), "s"(bt), "s"(lens), "s"(ws), "s"(out), "s"(nbps), "s"(kvbytes));
  const __amdgpu_buffer_rsrc_t wsr = __builtin_amdgcn_make_buffer_rsrc(ws, 0, WS_USED, 0x00020000);
  const int skv = nbps * BS;

  if (wave == 0) {
    const int mybox = (kvh * NS + sp) * BOX, fline = FLAGS_OFF + kvh * NS * FSTR;
    const int piece = lane & 3;
    const int moff = (lane >> 2) * MSG + piece * 16, toff = (lane >> 2) * MSG + 64;
    const int woff = (lane & (NS - 1)) * FSTR + sp * 4;
    const int roff = piece < 2 ? sp * FSTR + ((lane >> 2) + 16 * piece) * 4 : (int)OOB;
    const unsigned snap = __builtin_amdgcn_raw_buffer_load_b32(wsr, woff, fline, AUX_SC1);
    unsigned rec = __builtin_amdgcn_raw_buffer_load_b32(wsr, roff, fline, AUX_SC1);
    u32x4 iv[NS / 16];
    u32x2 it[NS / 16];
#pragma unroll
    for (int i = 0; i < NS / 16; ++i) {
      iv[i] = __builtin_amdgcn_raw_buffer_load_b128(wsr, moff + i * 16 * MSG, mybox, AUX_SC1);
      it[i] = __builtin_amdgcn_raw_buffer_load_b64(wsr, toff + i * 16 * MSG, mybox, AUX_SC1);
    }
    const int len_raw = lens[0];
    asm volatile("s_waitcnt vmcnt(0)" ::: "memory");
    unsigned old0;
    {
      unsigned bits[NS / 16];
#pragma unroll
      for (int i = 0; i < NS / 16; ++i) {
        const u32x4 x = ~iv[i] & 1u;
        unsigned y = (x[0] | x[1] << 1 | x[2] << 2 | x[3] << 3) << (4 * piece) | (~it[i][0] & 1u) << 16 | (~it[i][1] & 1u) << 17;
        y |= dppu<0xB1>(y);
        y |= dppu<0x4E>(y);
        bits[i] = y;
      }
      const unsigned found = (unsigned)__ballot((snap >> 18) & 1u);
      old0 = (unsigned)(__ballot((rec >> 18) & 1u) >> ((lane & 15) * 4 + ((lane >> 4) & 1))) & 1u;
      rec = (found >> ((lane >> 2) + 16 * (piece & 1))) << 31 | (rec & 0x7ffc0000u) | (piece == 0 ? bits[0] : bits[1]);
    }
    const int len = len_raw <= 0 ? skv : min(len_raw, skv);
    const int nunits = (len + 3) >> 2;
    const int nmax = (nunits + WAVES_PER_HEAD - 1) / WAVES_PER_HEAD;
    unsigned cur = snap;
    auto announce = [&]() { rec = (rec & 0x8003ffffu) | ((rec + (1u << 18)) & 0x7ffc0000u); __builtin_amdgcn_raw_buffer_store_b32(rec, wsr, roff, fline, AUX_SC1); };
    auto all_ready = [&]() -> bool {
      asm volatile("" ::: "memory");
      cur = __builtin_amdgcn_raw_buffer_load_b32(wsr, woff, fline, AUX_SC1);
      return __all(cur != snap);
    };
#pragma unroll 1
    for (int a = 0; a < 2; ++a) {
#pragma unroll 1
      for (int i = min(nmax, 2 * NU); i > 0; --i) __builtin_amdgcn_s_sleep(FLAG_DELAY);
      if (a == 0) announce();
    }
#pragma unroll 1
    for (unsigned spins = 0; spins < SPIN_CAP; ++spins) {
      if (all_ready()) break;
      __builtin_amdgcn_s_sleep(8);
    }
    if (__any(cur >> 31 != old0)) announce();
    if (lane < NS) {
      u32x4* ph = reinterpret_cast<u32x4*>(lds + LDS_PH) + lane * 5;
#pragma unroll
      for (int k = 0; k < 5; ++k) ph[k] = u32x4{cur >> (4 * k), cur >> (4 * k + 1), cur >> (4 * k + 2), cur >> (4 * k + 3)} & 1u;
    }
    __syncthreads();
    __builtin_amdgcn_s_sleep(GATHER_DELAY);
    u32x4 v[NS / 16];
    u32x2 t[NS / 16];
#pragma unroll
    for (int i = 0; i < NS / 16; ++i) { v[i] = iv[i]; t[i] = it[i]; }
    auto landed = [&](int i) -> bool {
      const u32x4 x = v[i] ^ iv[i];
      const u32x2 y = t[i] ^ it[i];
      return ((x[0] & x[1] & x[2] & x[3] & y[0] & y[1]) & 1u) != 0u;
    };
#pragma unroll 1
    for (unsigned spins = 0; spins < SPIN_CAP; ++spins) {
      asm volatile("" ::: "memory");
#pragma unroll
      for (int i = 0; i < NS / 16; ++i)
        if (!landed(i)) {
          v[i] = __builtin_amdgcn_raw_buffer_load_b128(wsr, moff + i * 16 * MSG, mybox, AUX_SC1);
          t[i] = __builtin_amdgcn_raw_buffer_load_b64(wsr, toff + i * 16 * MSG, mybox, AUX_SC1);
        }
      bool ok = true;
#pragma unroll
      for (int i = 0; i < NS / 16; ++i) ok &= landed(i);
      if (__all(ok)) break;
      __builtin_amdgcn_s_sleep(1);
    }
#pragma unroll
    for (int i = 0; i < NS / 16; ++i) { v[i] &= ~1u; t[i] &= ~1u; }
    float M = __uint_as_float(t[0][0]);
#pragma unroll
    for (int i = 1; i < NS / 16; ++i) M = fmaxf(M, __uint_as_float(t[i][0]));
    M = fmaxf(M, dppf<ROR4>(M));
    M = fmaxf(M, dppf<ROR8>(M));
    M = xrow_max(M);
    float L = 0.f;
    f32x4 o = {0.f, 0.f, 0.f, 0.f};
#pragma unroll
    for (int i = 0; i < NS / 16; ++i) {
      const float wt = __builtin_amdgcn_exp2f((__uint_as_float(t[i][0]) - M) * C2);
      L = fmaf(wt, __uint_as_float(t[i][1]), L);
      o += wt * __builtin_bit_cast(f32x4, v[i]);
    }
    L += dppf<ROR4>(L); L += dppf<ROR8>(L); L = xrow_sum(L);
#pragma unroll
    for (int k = 0; k < 4; ++k) { float x = o[k]; x += dppf<ROR4>(x); x += dppf<ROR8>(x); o[k] = xrow_sum(x); }
    const float inv = __builtin_amdgcn_rcpf(L);
    const u32x2 res = {pk_bf16(o[0] * inv, o[1] * inv), pk_bf16(o[2] * inv, o[3] * inv)};
    if (lane < 4) *reinterpret_cast<u32x2*>(out + kvh * (G * D) + sp * 16 + lane * 4) = res;
  } else {
    constexpr int NL = 2 * NU;
    constexpr int CARRY = issued_after(NL - 1) - NL;
    static_assert(CARRY == issued_after(-1), "window schedule");
    unsigned sb[CARRY];
#pragma unroll
    for (int i = 0; i < CARRY; ++i) sb[i] = ((const CAS unsigned*)bt)[(unsigned)min((gw >> 2) + (WAVES_PER_HEAD / 4) * i, nbps - 1)];
    const int len_raw = ((const CAS int*)lens)[0];
    __builtin_amdgcn_sched_barrier(0);
    const int btv = bt[(unsigned)min(pgi, nbps - 1)];
    const int row = lane >> 4, n16 = lane & 15, bank = n16 >> 2, hh = n16 & 3;
    const u32x4 qf = *reinterpret_cast<const u32x4*>(q + (unsigned)((kvh * G + hh) * D + 32 * row + 8 * bank));
    const int shift = kvh * (D * 2) + (gw & 3) * (4 * TOK_BYTES);
    const __amdgpu_buffer_rsrc_t krs = __builtin_amdgcn_make_buffer_rsrc((void*)(kc + shift), 0, kvbytes - shift, 0x00020000);
    const __amdgpu_buffer_rsrc_t vrs = __builtin_amdgcn_make_buffer_rsrc((void*)(vc + shift), 0, kvbytes - shift, 0x00020000);
    const int koff = bank * TOK_BYTES + row * 64 + hh * 16;
    const int voff = row * TOK_BYTES + n16 * 16;
    u32x4 K[NU], V[NU];
    __builtin_amdgcn_sched_barrier(0);
#pragma unroll
    for (int i = 0; i < CARRY; ++i)
      K[i] = __builtin_amdgcn_raw_buffer_load_b128(krs, koff, sb[i] * PAGE_BYTES, KV_AUX);
    __builtin_amdgcn_sched_barrier(0);
    const bool allmask = len_raw <= 0;
    const int len = allmask ? skv : min(len_raw, skv);
    const int nunits = (len + 3) >> 2;
    const int n = (nunits > gw) ? (nunits - gw + WAVES_PER_HEAD - 1) / WAVES_PER_HEAD : 0;
    const int nr = (n + NU - 1) / NU;
    const unsigned offv = (gw + WAVES_PER_HEAD * lane < nunits) ? (unsigned)btv * PAGE_BYTES : OOB;
    const int tokl = 4 * (gw + WAVES_PER_HEAD * bank) + row;

    unsigned so[NU];
    auto issue = [&](int r, int i) {
      if (i < NU) K[i] = __builtin_amdgcn_raw_buffer_load_b128(krs, koff, so[i], KV_AUX);
      else if (i < NL) V[i - NU] = __builtin_amdgcn_raw_buffer_load_b128(vrs, voff, so[i - NU], KV_AUX);
      else K[i - NL] = __builtin_amdgcn_raw_buffer_load_b128(krs, koff, __builtin_amdgcn_readlane(offv, ((r + 1) * NU + i - NL) & 63), KV_AUX);
    };
    f32x4 acc[8];
#pragma unroll
    for (int j = 0; j < 8; ++j) acc[j] = f32x4{0.f, 0.f, 0.f, 0.f};
    float m = -INFINITY, l = 0.f;

    const int t = tid - 64, h = t >> 7, d = t & 127;
    LAS f32x4* a_dst = (LAS f32x4*)(lds + sw * WS + n16 * HS + 4 * row);
    LAS float* a_src = (LAS float*)(lds + h * HS + (d & 7) * JS + (d >> 3));
    LAS f32x4* a_ml = (LAS f32x4*)(lds + LDS_M + h * NW);
    LAS u32x4* a_ph = (LAS u32x4*)(lds + LDS_PH) + (t >> 4) * 5 + (n16 == 1 ? 4 : bank);
    int dsto = (hh == 0 || n16 == 1) ? (kvh * NS + (t >> 4)) * BOX + sp * MSG + (n16 == 1 ? 64 : bank * 16) : (int)OOB;
    asm volatile("" : "+v"(a_dst), "+v"(a_src), "+v"(a_ml), "+v"(a_ph), "+v"(dsto));
    for (int r = 0;; ++r) {
      const bool last = r + 1 >= nr;
#pragma unroll
      for (int k = 0; k < NU; ++k) so[k] = __builtin_amdgcn_readlane(offv, (r * NU + k) & 63);
      __builtin_amdgcn_sched_barrier(0);
      float s[R];
#pragma unroll
      for (int j = 0; j < R; ++j) {
        f32x4 d[4];
#pragma unroll
        for (int i = 0; i < 4; ++i) {
          if (4 * j + i < NU) {
            d[i] = mfma32(K[4 * j + i], qf, f32x4{0.f, 0.f, 0.f, 0.f});
            __builtin_amdgcn_sched_barrier(0);
#pragma unroll
            for (int x = issued_after(4 * j + i - 1); x < issued_after(4 * j + i); ++x) issue(r, x);
            __builtin_amdgcn_sched_barrier(0);
          } else {
            d[i] = d[0];
          }
        }
        float sc = diag_reduce(d[0], d[1], d[2], d[3]);
        sc = allmask ? 0.f : sc;
        const bool live = tokl < len - (r * NU + 4 * j) * (4 * WAVES_PER_HEAD) && (4 * j + 3 < NU || 4 * j + bank < NU);
        s[j] = live ? sc : -INFINITY;
      }
      float mx = s[0];
#pragma unroll
      for (int j = 1; j < R; ++j) mx = fmaxf(mx, s[j]);
      mx = fmaxf(mx, dppf<ROR4>(mx));
      mx = fmaxf(mx, dppf<ROR8>(mx));
      mx = xrow_max(mx);
      const float mn = fmaxf(m, mx);
      const float ms = (mn == -INFINITY) ? 0.f : mn;
      if (r > 0) {
        const float alpha = __builtin_amdgcn_exp2f((m - ms) * C2);
        l *= alpha;
#pragma unroll
        for (int j = 0; j < 8; ++j) acc[j] *= alpha;
      }
      m = mn;
      float p[R];
#pragma unroll
      for (int j = 0; j < R; ++j) { p[j] = __builtin_amdgcn_exp2f((s[j] - ms) * C2); l += p[j]; }
      u32x4 pb[3];
#pragma unroll
      for (int g = 0; g < 2; ++g) {
        const unsigned x = pk_bf16(p[2 * g], p[2 * g + 1]);
        pb[g] = u32x4{x, dppu<SHL4>(x), dppu<SHL8>(x), dppu<SHL12>(x)};
      }
      {
        const unsigned x = pk_bf16(p[4], dppf<SHL4>(p[4]));
        pb[2] = u32x4{x, NU > 18 ? dppu<SHL8>(x) : 0u, 0u, 0u};
      }
      __builtin_amdgcn_sched_barrier(0);
      u32x4 zq[8];
#pragma unroll
      for (int i = 0; i < NU; ++i) {
        asm volatile("" :: "v"(V[i]));
        __builtin_amdgcn_sched_barrier(0);
#pragma unroll
        for (int x = issued_after(NU + i - 1); x < issued_after(NU + i); ++x) issue(r, x);
        __builtin_amdgcn_sched_barrier(0);
        if (i == 0 && last) {
          float lt = l + dppf<ROR4>(l);
          lt += dppf<ROR8>(lt);
          lt = xrow_sum(lt);
          if (lane < G) { lds[LDS_L + lane * NW + sw] = lt; lds[LDS_M + lane * NW + sw] = fmaxf(m, -0x1p100f); }
        }
#pragma unroll
        for (int x = pv_done_after(i - 1); x < pv_done_after(i); ++x) {
          const int g = x >> 3, e = x & 7;
          const unsigned sel = (e & 1) ? 0x07060302u : 0x05040100u;
          u32x4 a;
          if (g < 2) {
#pragma unroll
            for (int k = 0; k < 4; ++k) a[k] = __builtin_amdgcn_perm(V[8 * g + 4 + k][e >> 1], V[8 * g + k][e >> 1], sel);
          } else {
            a = zq[e];
            a[0] = __builtin_amdgcn_perm(V[NU > 17 ? 17 : 16][e >> 1], V[16][e >> 1], sel);
            if (NU > 18) a[1] = __builtin_amdgcn_perm(V[NU > 19 ? 19 : 16][e >> 1], V[NU > 18 ? 18 : 16][e >> 1], sel);
          }
          acc[e] = mfma32(a, pb[g], acc[e]);
        }
        if (i == NU - 2) {
#pragma unroll
          for (int e = 0; e < 8; ++e) { zq[e] = u32x4{0u, 0u, 0u, 0u}; asm volatile("" : "+v"(zq[e])); }
        }
        __builtin_amdgcn_sched_barrier(0);
      }
      if (last) break;
    }
    if (n16 < G) {
#pragma unroll
      for (int e = 0; e < 8; ++e) a_dst[e * (JS / 4)] = acc[e];
    }
    __syncthreads();
    {
      const f32x4 m0 = a_ml[0], m1 = a_ml[1];
      float x[NW];
#pragma unroll
      for (int w = 0; w < NW; ++w) x[w] = a_src[w * WS];
      const f32x4 l0 = a_ml[(LDS_L - LDS_M) / 4], l1 = a_ml[(LDS_L - LDS_M) / 4 + 1];
      const u32x4 ph = *a_ph;
      __builtin_amdgcn_sched_barrier(0);
      const float M = max3(max3(m0[0], m0[1], m0[2]), max3(m0[3], m1[0], m1[1]), max3(m1[2], m1[3], m1[3]));
      float wt[NW];
#pragma unroll
      for (int w = 0; w < NW; ++w) wt[w] = __builtin_amdgcn_exp2f(((w < 4 ? m0[w & 3] : m1[w & 3]) - M) * C2);
      const float o = fmaf(wt[0], x[0], fmaf(wt[1], x[1], fmaf(wt[2], x[2], wt[3] * x[3]))) + fmaf(wt[4], x[4], fmaf(wt[5], x[5], fmaf(wt[6], x[6], wt[7] * x[7])));
      const float L = fmaf(wt[0], l0[0], fmaf(wt[1], l0[1], fmaf(wt[2], l0[2], wt[3] * l0[3]))) + fmaf(wt[4], l1[0], fmaf(wt[5], l1[1], fmaf(wt[6], l1[2], wt[7] * l1[3])));
      const unsigned ob = __float_as_uint(o);
      u32x4 v4 = {ob, dppu<0x55>(ob), dppu<0xAA>(ob), dppu<0xFF>(ob)};
      if (n16 == 1) v4 = u32x4{__float_as_uint(M), __float_as_uint(L), 0u, 0u};
      __builtin_amdgcn_raw_buffer_store_b128((v4 & ~1u) | ph, wsr, dsto, 0, AUX_SC1);
    }
  }
}

extern "C" void kernel_launch(void* const* d_in, const int* in_sizes, int n_in,
                              void* d_out, int out_size, void* d_ws, size_t ws_size,
                              hipStream_t stream) {
  const int nbps = in_sizes[3];
  pa_decode_kernel<<<NWG, NTHR, 0, stream>>>((const unsigned short*)d_in[0], (const unsigned char*)d_in[1], (const unsigned char*)d_in[2],
                                             (const int*)d_in[3], (const int*)d_in[4], (unsigned char*)d_ws, (unsigned short*)d_out,
                                             nbps, in_sizes[1] * 2);
}
```
